```python
import torch

def _softmax(x, axis=-1):
    return torch.softmax(x, dim=axis)
def _gelu(x):
    return torch.nn.functional.gelu(x, approximate="tanh")
def _silu(x):
    return torch.nn.functional.silu(x)
def _sigmoid(x):
    return torch.sigmoid(x)
def _layernorm(x, axis=-1, eps=1e-5):
    m = x.mean(dim=axis, keepdim=True)
    v = x.var(dim=axis, keepdim=True, unbiased=False)
    return (x - m) / torch.sqrt(v + eps)
def _scatter_add(tgt, idx, src, axis=0):
    return tgt.clone().index_add_(axis, idx, src)
def _segment_sum(src, seg, n):
    out = torch.zeros((n,) + src.shape[1:], dtype=src.dtype)
    return out.index_add_(0, seg, src)
def _pad(x, pw):
    flat = tuple(v for pair in reversed(pw) for v in pair)
    return torch.nn.functional.pad(x, flat)

kernel_name = 'concat_heads__ssm_conv_h3__compressed_kv_cla__prompt'

def setup_inputs():

    _w = (lambda a: (lambda t: t.to(torch.bfloat16) if t.is_floating_point() else t)(torch.as_tensor(a)))
    def _A_setup():
        B, S, D = 1, 2048, 576
        ks = 4
        return {
            "q": _w((lambda *s: torch.randn(*s, dtype=torch.bfloat16) * 0.05)(B, S, D)),
            "k": _w((lambda *s: torch.randn(*s, dtype=torch.bfloat16) * 0.1)(B, S, D)),
            "v": _w((lambda *s: torch.randn(*s, dtype=torch.bfloat16) * 0.1)(B, S, D)),
            "shift_k": _w((lambda *s: torch.randn(*s, dtype=torch.bfloat16) * 0.1)(D, ks) * 0.5),
            "ssm_k": _w((lambda *s: torch.randn(*s, dtype=torch.bfloat16) * 0.1)(D, S) * (1.0 / S ** 0.5)),
            "ssm_D": _w((lambda *s: torch.randn(*s, dtype=torch.bfloat16) * 0.1)(D)),
        }
    _A_in = _A_setup()

    def _B_setup():
        B, H, Sq, Sk, D, M = 1, 32, 2048, 2048, 128, 576
        Dc = 4 * D
        return {
            "x": _w((lambda *s: torch.randn(*s, dtype=torch.bfloat16) * 3.2)(B, Sq, M)),
            "c_kv": _w((lambda *s: torch.randn(*s, dtype=torch.bfloat16) * 0.05)(B, Sk, Dc)),
            "w_q": _w((lambda *s: torch.randn(*s, dtype=torch.bfloat16) * 3.2)(M, H, D) * (M ** -0.5)),
            "w_uk": _w((lambda *s: torch.randn(*s, dtype=torch.bfloat16) * 3.2)(Dc, H, D) * (Dc ** -0.5)),
            "w_uv": _w((lambda *s: torch.randn(*s, dtype=torch.bfloat16) * 0.05)(Dc, H, D) * (Dc ** -0.5)),
            "w_o": _w((lambda *s: torch.randn(*s, dtype=torch.bfloat16) * 0.05)(H, D, M) * ((H * D) ** -0.5)),
        }
    _B_in = _B_setup()

    return {
        "A_q": _A_in["q"],
        "A_k": _A_in["k"],
        "A_v": _A_in["v"],
        "A_shift_k": _A_in["shift_k"],
        "A_ssm_k": _A_in["ssm_k"],
        "A_ssm_D": _A_in["ssm_D"],
        "B_x": _B_in["x"],
        "B_c_kv": _B_in["c_kv"],
        "B_w_q": _B_in["w_q"],
        "B_w_uk": _B_in["w_uk"],
        "B_w_uv": _B_in["w_uv"],
        "B_w_o": _B_in["w_o"],
    }

def reference(A_q, A_k, A_v, A_shift_k, A_ssm_k, A_ssm_D, B_x, B_c_kv, B_w_q, B_w_uk, B_w_uv, B_w_o):
    def _A_ref(q, k, v, shift_k, ssm_k, ssm_D):
        B, S, D = q.shape
        ks = shift_k.shape[-1]
        k_pad = _pad(k, ((0, 0), (ks - 1, 0), (0, 0)))
        k_shift = torch.zeros_like(k)
        for t in range(ks):
            k_shift = k_shift + k_pad[:, t:t + S, :] * shift_k[:, ks - 1 - t][None, None, :]
        kv = k_shift * v
        L = 2 * S
        kv_pad = _pad(kv, ((0, 0), (0, S), (0, 0)))
        kf_pad = _pad(ssm_k, ((0, 0), (0, S)))
        kv_f = (lambda x, axes=(-2,-1): torch.fft.rfft2(x.float(), dim=axes))(kv_pad[:, None, :, :], axes=(1, 2))
        kf_f = (lambda x, axes=(-2,-1): torch.fft.rfft2(x.float(), dim=axes))(kf_pad[:, None, :], axes=(1, 2))
        kf_f = (lambda x, *d: x.permute(*d))(kf_f, 1, 2, 0)
        y_f = kv_f * kf_f[None, :, :, :]
        y = (lambda x, s, axes=(-2,-1): torch.fft.irfft2(x, s=s, dim=axes).to(torch.bfloat16))(y_f, (1, L), axes=(1, 2))[:, 0, :S, :]
        y = y + kv * ssm_D[None, None, :]
        return q * y

    def _B_ref(x, c_kv, w_q, w_uk, w_uv, w_o):
        B, Sq, M = x.shape[0], x.shape[1], x.shape[2]
        Sk = c_kv.shape[1]
        H, D = w_q.shape[1], w_q.shape[2]
        q = torch.einsum("bqm,mhd->bhqd", x, w_q)
        k = torch.einsum("bkc,chd->bhkd", c_kv, w_uk)
        v = torch.einsum("bkc,chd->bhkd", c_kv, w_uv)
        scores = torch.einsum("bhqd,bhkd->bhqk", q, k) * (D ** -0.5)
        off = Sk - Sq
        qi = torch.arange(Sq)[:, None]
        kj = torch.arange(Sk)[None, :]
        cmask = kj <= (qi + off)
        scores = torch.where(cmask[None, None], scores, -1e9)
        p = _softmax(scores, axis=-1)
        ctx = torch.einsum("bhqk,bhkd->bhqd", p, v)
        return torch.einsum("bhqd,hdm->bqm", ctx, w_o)

    a = _A_ref(A_q, A_k, A_v, A_shift_k, A_ssm_k, A_ssm_D)
    b = _B_ref(B_x, B_c_kv, B_w_q, B_w_uk, B_w_uv, B_w_o)
    return torch.concatenate([a, b], axis=-1)
```

```cpp
#include <hip/hip_runtime.h>
#include <hip/hip_bf16.h>
#include <stdint.h>

typedef __attribute__((ext_vector_type(8))) __bf16 bf16x8;
typedef __attribute__((ext_vector_type(2))) __bf16 bf16x2;
typedef __attribute__((ext_vector_type(2))) float f32x2;
typedef __attribute__((ext_vector_type(4))) float f32x4;
typedef __attribute__((ext_vector_type(16))) float f32x16;
typedef __attribute__((ext_vector_type(4))) short s16x4;
typedef __attribute__((ext_vector_type(8))) short s16x8;
typedef __attribute__((ext_vector_type(4))) unsigned u32x4;
typedef __attribute__((ext_vector_type(2))) unsigned u32x2;
#define LAS __attribute__((address_space(3)))
#define GAS __attribute__((address_space(1)))
typedef LAS char* ldsp;
typedef __amdgpu_buffer_rsrc_t rsrc_t;

constexpr int S_LEN = 2048, DA = 576, NH = 32, HD = 128, DC = 512, OUTW = 1152;
constexpr int LDS_BYTES = 163840;
constexpr size_t WS_EPOCH = 0;
constexpr size_t WS_FKV = 4096;
constexpr size_t WS_FATT = 8192;
constexpr size_t WS_FPART = 12288;
constexpr size_t WS_FA2 = 20480;
constexpr size_t WS_KV = 1u << 20;
constexpr size_t WS_CTX = WS_KV + (32u << 20);
constexpr size_t WS_PART = WS_CTX + (16u << 20);
constexpr size_t WS_KVR = WS_PART + (20u << 20);
constexpr size_t WS_Y2 = WS_KVR + (4u << 20);
constexpr int KVR_PITCH = 2304;

template <bool B> struct BoolC { static constexpr bool value = B; };
template <int V> struct IntC { static constexpr int value = V; };

template <int I, int N, class Fn> __device__ __forceinline__ void static_for(Fn&& f) {
  if constexpr (I < N) { f(IntC<I>{}); static_for<I + 1, N>(f); }
}

struct Params {
  const __bf16 *aq, *ak, *av, *ashift, *assm, *aD, *x, *ckv, *wq, *wuk, *wuv, *wo;
  __bf16* out;
  char* ws;
};

__device__ __forceinline__ rsrc_t mk_rsrc(const void* p, unsigned bytes) {
  return __builtin_amdgcn_make_buffer_rsrc((void*)p, (short)0, (int)bytes, 0x00020000);
}
__device__ __forceinline__ void dma16(unsigned ldsoff, unsigned voff, rsrc_t r, unsigned soff) {
  asm volatile("s_mov_b32 m0, %0\n\ts_nop 3\n\tbuffer_load_dwordx4 %1, %2, %3 offen lds"
               :: "s"(ldsoff), "v"(voff), "s"(r), "s"(soff) : "memory");
}
__device__ __forceinline__ void touch4(unsigned ldsoff, unsigned voff, rsrc_t r) {
  asm volatile("s_mov_b32 m0, %0\n\ts_nop 3\n\tbuffer_load_dword %1, %2, 0 offen lds" :: "s"(ldsoff), "v"(voff), "s"(r) : "memory");
}
__device__ __forceinline__ void touchv(unsigned& v, unsigned voff, rsrc_t r) {
  asm volatile("buffer_load_dword %0, %1, %2, 0 offen" : "+v"(v) : "v"(voff), "s"(r) : "memory");
}
__device__ __forceinline__ void dma16_dev(unsigned ldsoff, unsigned voff, rsrc_t r, unsigned soff) {
  asm volatile("s_mov_b32 m0, %0\n\ts_nop 3\n\tbuffer_load_dwordx4 %1, %2, %3 offen sc1 lds"
               :: "s"(ldsoff), "v"(voff), "s"(r), "s"(soff) : "memory");
}
template <int U, int OFF>
__device__ __forceinline__ void dma16_if(int nwin, unsigned ldsoff, unsigned voff, rsrc_t r, unsigned soff) {
  asm volatile("s_cmp_le_i32 %4, %5\n\ts_cbranch_scc1 1f\n\ts_mov_b32 m0, %0\n\ts_nop 3\n\t"
               "buffer_load_dwordx4 %1, %2, %3 offen offset:%6 sc1 lds\n1:"
               :: "s"(ldsoff), "v"(voff), "s"(r), "s"(soff), "s"(nwin), "i"(U), "i"(OFF) : "memory", "scc");
}
__device__ __forceinline__ void dma16_on(int on, unsigned ldsoff, unsigned voff, rsrc_t r, unsigned soff) {
  asm volatile("s_cmp_eq_u32 %4, 0\n\ts_cbranch_scc1 1f\n\ts_mov_b32 m0, %0\n\ts_nop 3\n\t"
               "buffer_load_dwordx4 %1, %2, %3 offen lds\n1:"
               :: "s"(ldsoff), "v"(voff), "s"(r), "s"(soff), "s"(on) : "memory", "scc");
}
__device__ __forceinline__ void wait_vm_units(int units) {
  asm volatile("s_cmp_gt_i32 %0, 2\n\ts_cbranch_scc1 3f\n\ts_cmp_eq_u32 %0, 2\n\ts_cbranch_scc1 2f\n\t"
               "s_cmp_eq_u32 %0, 1\n\ts_cbranch_scc1 1f\n\ts_waitcnt vmcnt(0)\n1:\n\ts_waitcnt vmcnt(4)\n2:\n\t"
               "s_waitcnt vmcnt(8)\n3:\n\ts_waitcnt vmcnt(12)" :: "s"(units) : "memory", "scc");
}
template <int N> __device__ __forceinline__ void wait_vm() { asm volatile("s_waitcnt vmcnt(%0)" :: "i"(N) : "memory"); }
__device__ __forceinline__ void wait_lgkm0() { asm volatile("s_waitcnt lgkmcnt(0)" ::: "memory"); }
__device__ __forceinline__ void bar() { __builtin_amdgcn_s_barrier(); asm volatile("" ::: "memory"); }
__device__ __forceinline__ bf16x8 lds_b128(ldsp p) { return *(const LAS bf16x8*)p; }
__device__ __forceinline__ bf16x8 lds_tr2(ldsp p0, ldsp p1) {
  s16x4 a = __builtin_amdgcn_ds_read_tr16_b64_v4i16((LAS s16x4*)p0);
  s16x4 b = __builtin_amdgcn_ds_read_tr16_b64_v4i16((LAS s16x4*)p1);
  s16x8 r = __builtin_shufflevector(a, b, 0, 1, 2, 3, 4, 5, 6, 7);
  return __builtin_bit_cast(bf16x8, r);
}
__device__ __forceinline__ unsigned pk2(float a, float b) {
  f32x2 v = {a, b};
  return __builtin_bit_cast(unsigned, __builtin_convertvector(v, bf16x2));
}
__device__ __forceinline__ f32x4 mfma16(bf16x8 a, bf16x8 b, f32x4 c) { return __builtin_amdgcn_mfma_f32_16x16x32_bf16(a, b, c, 0, 0, 0); }
__device__ __forceinline__ f32x16 mfma32(bf16x8 a, bf16x8 b, f32x16 c) { return __builtin_amdgcn_mfma_f32_32x32x16_bf16(a, b, c, 0, 0, 0); }
__device__ __forceinline__ void st_wt(u32x4 v, rsrc_t r, unsigned off) { __builtin_amdgcn_raw_buffer_store_b128(v, r, off, 0, 16); }

typedef GAS unsigned long long gu64;
__device__ __forceinline__ bf16x8 ldg16(rsrc_t r, unsigned voff, unsigned soff) {
  return __builtin_bit_cast(bf16x8, __builtin_amdgcn_raw_buffer_load_b128(r, voff, soff, 0));
}
__device__ __forceinline__ bf16x8 ldg16_dev(rsrc_t r, unsigned voff, unsigned soff) {
  return __builtin_bit_cast(bf16x8, __builtin_amdgcn_raw_buffer_load_b128(r, voff, soff, 16));
}
__device__ __forceinline__ float bf2f(unsigned short v) { return __builtin_bit_cast(float, (unsigned)v << 16); }
__device__ __forceinline__ float bflo(unsigned v) { return __builtin_bit_cast(float, v << 16); }
__device__ __forceinline__ float bfhi(unsigned v) { return __builtin_bit_cast(float, v & 0xffff0000u); }
__device__ __forceinline__ float rbf(float x) { return (float)(__bf16)x; }
__device__ __forceinline__ void raise_flag(char* ws, size_t flag, unsigned long long epoch) {
  if (threadIdx.x == 0) __hip_atomic_store((gu64*)(ws + flag), epoch, __ATOMIC_RELAXED, __HIP_MEMORY_SCOPE_AGENT);
}
__device__ __forceinline__ void publish(char* ws, size_t flag, unsigned long long epoch) {
  wait_vm<0>();
  bar();
  raise_flag(ws, flag, epoch);
}
__device__ __forceinline__ void poll_flags(char* ws, size_t first, int n, unsigned long long epoch, int lane) {
  gu64* f = (gu64*)(ws + first) + (lane < n ? lane : 0);
  for (unsigned spins = 0; spins < (1u << 22); ++spins) {
    const unsigned long long v = __hip_atomic_load(f, __ATOMIC_RELAXED, __HIP_MEMORY_SCOPE_AGENT);
    if (__all(v == epoch)) break;
    __builtin_amdgcn_s_sleep(1);
  }
}
__device__ __forceinline__ void await_flags(char* ws, size_t first, int n, unsigned long long epoch, int wid, int lane) {
  if (wid == 0) poll_flags(ws, first, n, epoch, lane);
  bar();
}


struct KvOut { u32x4 v[16]; unsigned base; };
template <bool ISV, class Pre, class Mid>
__device__ __forceinline__ void kv_phase(KvOut& out, const Params& p, ldsp lds, int wid, int lane, int head0, int mt, Pre&& pre, Mid&& mid) {
  const rsrc_t rW = mk_rsrc(ISV ? p.wuv : p.wuk, DC * 4096 * 2);
  const rsrc_t rC = mk_rsrc(p.ckv, S_LEN * DC * 2);
  const int wt = wid & 1, wn = wid >> 1;
  const int r32 = lane & 31, hh = lane >> 5;
  const unsigned vC0 = (lane >> 3) * 1024 + (((lane & 7) ^ (lane >> 4)) << 4);
  const unsigned vC1 = vC0 ^ 64;
  const unsigned vW0 = hh * 8192 + (((r32 >> 2) ^ hh) << 6) + ((lane & 3) << 4);
  const unsigned vW1 = hh * 8192 + (((r32 >> 2) ^ (2 + hh)) << 6) + ((lane & 3) << 4);
  const unsigned sC = (mt * 256 + 32 * wid) * 1024;
  const unsigned sW = (8 * wid) * 8192 + head0 * 256;
  auto dmaC = [&](int kk, unsigned slot, int i) { dma16(slot + wid * 4096 + i * 1024, (i & 1) ? vC1 : vC0, rC, sC + i * 8192 + kk * 128); };
  auto dmaW = [&](int kk, unsigned slot, int i) { dma16(slot + wid * 4096 + i * 1024, (i & 1) ? vW1 : vW0, rW, sW + (kk * 64 + 2 * i) * 8192); };
  const unsigned aC = (wt * 128 + r32) * 128 + ((hh ^ ((r32 >> 1) & 7)) << 4);
  const int q4 = (lane & 15) >> 2;
  unsigned aW[2];
#pragma unroll
  for (int nb = 0; nb < 2; ++nb) aW[nb] = (8 * hh + q4) * 512 + (((wn * 2 + nb) ^ q4) << 6) + ((lane >> 4) & 1) * 32 + (lane & 3) * 8;

  f32x16 acc[4][2];
#pragma unroll
  for (int i = 0; i < 4; ++i)
#pragma unroll
    for (int j = 0; j < 2; ++j)
#pragma unroll
      for (int e = 0; e < 16; ++e) acc[i][j][e] = 0.f;

  pre();
#pragma unroll
  for (int i = 0; i < 4; ++i) dmaC(0, 0, i);
#pragma unroll
  for (int i = 0; i < 4; ++i) dmaW(0, 32768, i);
#pragma unroll
  for (int i = 0; i < 4; ++i) dmaC(1, 65536, i);
#pragma unroll
  for (int i = 0; i < 4; ++i) dmaW(1, 98304, i);
  mid();
  wait_lgkm0(); wait_vm<10>(); bar();
#pragma unroll
  for (int i = 0; i < 4; ++i) dmaC(2, 131072, i);

  bf16x8 cf[2][4], wf[2][2];
  auto rd = [&](int buf, unsigned sc, unsigned sw, int ks, int part) {
    if (part == 0) {
#pragma unroll
      for (int nb = 0; nb < 2; ++nb) { ldsp bw = lds + (sw + aW[nb]) + ks * 8192; wf[buf][nb] = lds_tr2(bw, bw + 2048); }
    } else {
      ldsp bc = lds + (sc + (aC ^ (ks * 32)));
      cf[buf][2 * part - 2] = lds_b128(bc + (2 * part - 2) * 4096);
      cf[buf][2 * part - 1] = lds_b128(bc + (2 * part - 1) * 4096);
    }
  };
  auto mm = [&](int buf, int tb) {
#pragma unroll
    for (int nb = 0; nb < 2; ++nb)
      acc[tb][nb] = ISV ? mfma32(cf[buf][tb], wf[buf][nb], acc[tb][nb]) : mfma32(wf[buf][nb], cf[buf][tb], acc[tb][nb]);
  };
  unsigned sc = 0, sw = 32768, sfree = 0;
  rd(0, sc, sw, 0, 0); rd(0, sc, sw, 0, 1); rd(0, sc, sw, 0, 2);
  const rsrc_t rX = mk_rsrc(p.x, S_LEN * DA * 2), rQ = mk_rsrc(p.wq, DA * 4096 * 2);
  const unsigned wg = (blockIdx.x >> 3) * 512 + wid * 64 + lane;
  const unsigned wq = wg < 9216 ? (wg >> 4) * 8192 + (blockIdx.x & 7) * 1024 + (wg & 15) * 64 : 0x80000000u;
  unsigned warm = 0;
  auto step = [&](int k, auto IC, auto IW, auto RD) {
#pragma unroll
    for (int ks = 0; ks < 3; ++ks) {
#pragma unroll
      for (int tb = 0; tb < 4; ++tb) {
        if (tb < 3) rd((ks + 1) & 1, sc, sw, ks + 1, tb);
        if constexpr (IC.value) { if (ks >= 1 && (tb & 1)) dmaC(k + 2, sfree, (ks - 1) * 2 + (tb >> 1)); }
        if constexpr (IC.value) { if (ks == 2 && tb == 3) touchv(warm, k == 1 ? wq : (wg + (k - 2) * 16384) * 64, k == 1 ? rQ : rX); }
        mm(ks & 1, tb);
        __builtin_amdgcn_sched_barrier(0);
      }
    }
    wait_lgkm0();
    if constexpr (IC.value) wait_vm<5>(); else if constexpr (IW.value) wait_vm<4>(); else wait_vm<0>();
    bar();
    const unsigned oc = sc;
    sfree = sw;
    sc += 65536; if (sc >= 163840) sc -= 163840;
    sw += 65536; if (sw >= 163840) sw -= 163840;
    __builtin_amdgcn_sched_barrier(0);
#pragma unroll
    for (int tb = 0; tb < 4; ++tb) {
      if constexpr (RD.value) { if (tb < 3) rd(0, sc, sw, 0, tb); }
      if constexpr (IW.value) dmaW(k + 2, oc, tb);
      mm(1, tb);
      __builtin_amdgcn_sched_barrier(0);
    }
  };
  using T = BoolC<true>; using F = BoolC<false>;
  step(0, F{}, T{}, T{});
#pragma unroll 1
  for (int k = 1; k < 6; ++k) step(k, T{}, T{}, T{});
  step(6, F{}, F{}, T{});
  asm volatile("" :: "v"(warm));
  step(7, F{}, F{}, F{});
  const int head = head0 + (wn >> 1);
  out.base = (head * 32 + mt * 4 + wt * 2) * 32768 + (ISV ? 16384 : 0) + (wn & 1) * 4096 + lane * 16;
#pragma unroll
  for (int tb = 0; tb < 4; ++tb)
#pragma unroll
    for (int nb = 0; nb < 2; ++nb)
#pragma unroll
      for (int s = 0; s < 2; ++s) {
        const f32x16& a = acc[tb][nb];
        out.v[tb * 4 + nb * 2 + s] = u32x4{pk2(a[8 * s], a[8 * s + 1]), pk2(a[8 * s + 2], a[8 * s + 3]),
                                           pk2(a[8 * s + 4], a[8 * s + 5]), pk2(a[8 * s + 6], a[8 * s + 7])};
      }
}

template <class Pre, class Mid>
__device__ __forceinline__ void kv_dispatch(KvOut& out, const Params& p, ldsp lds, int wid, int lane, int b, Pre&& pre, Mid&& mid) {
  const int x = b & 7, s = b >> 3;
  const int pan = s & 3, mt = s >> 2;
  const int head0 = 4 * x + 2 * (pan & 1);
  if (pan < 2) kv_phase<false>(out, p, lds, wid, lane, head0, mt, pre, mid);
  else kv_phase<true>(out, p, lds, wid, lane, head0, mt, pre, mid);
}

__device__ __forceinline__ void dma16x4(unsigned ldsoff, unsigned voff, rsrc_t r, unsigned soff) {
  asm volatile("s_mov_b32 m0, %0\n\ts_nop 3\n\tbuffer_load_dwordx4 %1, %2, %3 offen sc1 lds\n\t"
               "buffer_load_dwordx4 %1, %2, %3 offen offset:1024 sc1 lds\n\tbuffer_load_dwordx4 %1, %2, %3 offen offset:2048 sc1 lds\n\t"
               "buffer_load_dwordx4 %1, %2, %3 offen offset:3072 sc1 lds"
               :: "s"(ldsoff), "v"(voff), "s"(r), "s"(soff) : "memory");
}
__device__ __forceinline__ unsigned opaque_s(unsigned v) { asm volatile("" : "+s"(v)); return v; }
__device__ __forceinline__ int opaque_v(int v) { asm volatile("" : "+v"(v)); return v; }
__device__ __forceinline__ float ex2(float x) { return __builtin_amdgcn_exp2f(x); }
__device__ __forceinline__ float xhalf(float v) { return __shfl_xor(v, 32); }
__device__ __forceinline__ bf16x8 pack8(const f32x16& a, int s, float c) {
  u32x4 v = {pk2(a[8 * s] * c, a[8 * s + 1] * c), pk2(a[8 * s + 2] * c, a[8 * s + 3] * c),
             pk2(a[8 * s + 4] * c, a[8 * s + 5] * c), pk2(a[8 * s + 6] * c, a[8 * s + 7] * c)};
  return __builtin_bit_cast(bf16x8, v);
}
__device__ __forceinline__ bf16x8 pack8n(const f32x16& a, int s) {
  u32x4 v = {pk2(a[8 * s], a[8 * s + 1]), pk2(a[8 * s + 2], a[8 * s + 3]), pk2(a[8 * s + 4], a[8 * s + 5]), pk2(a[8 * s + 6], a[8 * s + 7])};
  return __builtin_bit_cast(bf16x8, v);
}

struct AttnState {
  f32x16 O[4];
  f32x16 negm;
  bf16x8 q[8];
  float l, m;
};
#define SB() __builtin_amdgcn_sched_barrier(0)

__device__ __forceinline__ constexpr int slot_off(int s) {
  return s < 16 ? s * 1024 : 16384 + ((s - 16) >> 3) * 8192 + (s & 3) * 2048 + ((s >> 2) & 1) * 1024;
}
__device__ __forceinline__ unsigned pk2e(f32x16& S, int i, float& l) {
  const float a = __builtin_amdgcn_exp2f(S[i]), b = __builtin_amdgcn_exp2f(S[i + 1]);
  l += a; l += b;
  return pk2(a, b);
}
__device__ __forceinline__ unsigned pk2e(f32x16& S, int i, f32x2& l2) {
  const f32x2 e = {__builtin_amdgcn_exp2f(S[i]), __builtin_amdgcn_exp2f(S[i + 1])};
  l2 += e;
  return pk2(e[0], e[1]);
}
template <class Sync, class Dma>
__device__ __forceinline__ void unit_fast(AttnState& st, ldsp t, bf16x8& F0, bf16x8& F1, bf16x8& F2, Sync&& sync, Dma&& dma) {
  bf16x8 F[35];
  F[0] = F0; F[1] = F1; F[2] = F2;
  f32x16 S0, S1;
  u32x4 P0[2], P1[2];
  f32x2 l2 = {st.l, 0.f};
  ldsp tn = t;
  static_for<0, 32>([&](auto SI) {
    constexpr int s = SI.value;
    if constexpr (s == 29) { wait_lgkm0(); tn = sync(); SB(); }
    if constexpr (s < 23) F[s + 3] = lds_b128(t + slot_off(s + 3));
    else if constexpr (s == 23) F[26] = lds_b128(t + slot_off(26));
    else if constexpr (s == 24) { F[27] = lds_b128(t + slot_off(27)); F[28] = lds_b128(t + slot_off(28)); }
    else if constexpr (s == 25) { F[29] = lds_b128(t + slot_off(29)); F[30] = lds_b128(t + slot_off(30)); }
    else if constexpr (s == 26) F[31] = lds_b128(t + slot_off(31));
    else if constexpr (s >= 29) F[s + 3] = lds_b128(tn + slot_off(s - 29));
    if constexpr (s == 29) dma(IntC<0>{});
    if constexpr (s == 31) dma(IntC<1>{});
    if constexpr (s < 12 && (s & 1)) dma(IntC<2 + (s >> 1)>{});
    if constexpr (s < 8) S0 = mfma32(F[s], st.q[s], s == 0 ? st.negm : S0);
    else if constexpr (s < 16) S1 = mfma32(F[s], st.q[s - 8], s == 8 ? st.negm : S1);
    else {
      const u32x4 pp = s < 24 ? P0[(s >> 2) & 1] : P1[(s >> 2) & 1];
      st.O[s & 3] = mfma32(F[s], __builtin_bit_cast(bf16x8, pp), st.O[s & 3]);
    }
    if constexpr (s >= 9 && s <= 12) P0[0][s - 9] = pk2e(S0, 2 * (s - 9), l2);
    if constexpr (s >= 13 && s <= 15) P0[1][s - 13] = pk2e(S0, 8 + 2 * (s - 13), l2);
    if constexpr (s == 16) P0[1][3] = pk2e(S0, 14, l2);
    if constexpr (s >= 17 && s <= 20) P1[0][s - 17] = pk2e(S1, 2 * (s - 17), l2);
    if constexpr (s >= 21 && s <= 24) P1[1][s - 21] = pk2e(S1, 8 + 2 * (s - 21), l2);
    SB();
  });
  st.l = l2[0] + l2[1];
  F0 = F[32]; F1 = F[33]; F2 = F[34];
}
__device__ __forceinline__ f32x16 qk_block(const AttnState& st, ldsp kb) {
  bf16x8 F[8];
  f32x16 c;
#pragma unroll
  for (int i = 0; i < 16; ++i) c[i] = 0.f;
#pragma unroll
  for (int s = 0; s < 3; ++s) F[s] = lds_b128(kb + s * 1024);
#pragma unroll
  for (int s = 0; s < 8; ++s) {
    if (s + 3 < 8) F[s + 3] = lds_b128(kb + (s + 3) * 1024);
    c = mfma32(F[s], st.q[s], c);
    SB();
  }
  return c;
}
__device__ __forceinline__ void pv_block(AttnState& st, ldsp vb, f32x16& S, float mn) {
  bf16x8 F[8];
#pragma unroll
  for (int s = 0; s < 3; ++s) F[s] = lds_b128(vb + (s & 3) * 2048 + (s >> 2) * 1024);
  u32x4 P[2];
#pragma unroll
  for (int i = 0; i < 16; ++i) S[i] -= mn;
#pragma unroll
  for (int i = 0; i < 8; ++i) P[i >> 2][i & 3] = pk2e(S, 2 * i, st.l);
  SB();
#pragma unroll
  for (int s = 0; s < 8; ++s) {
    if (s + 3 < 8) F[s + 3] = lds_b128(vb + ((s + 3) & 3) * 2048 + ((s + 3) >> 2) * 1024);
    st.O[s & 3] = mfma32(F[s], __builtin_bit_cast(bf16x8, P[s >> 2]), st.O[s & 3]);
    SB();
  }
}
__device__ __forceinline__ void unit_gen(AttnState& st, ldsp t, int lane, bool upd, bool diag0, int mode1) {
  const int ql = lane & 31, k0 = 4 * (lane >> 5);
  f32x16 S0 = qk_block(st, t), S1 = S0;
  if (mode1 != 2) S1 = qk_block(st, t + 8192);
  if (diag0) {
#pragma unroll
    for (int i = 0; i < 16; ++i) S0[i] = ((i & 3) + 8 * (i >> 2) + k0 <= ql) ? S0[i] : -1e30f;
  }
  if (mode1 == 1) {
#pragma unroll
    for (int i = 0; i < 16; ++i) S1[i] = ((i & 3) + 8 * (i >> 2) + k0 <= ql) ? S1[i] : -1e30f;
  }
  if (upd) {
    float mx = S0[0];
#pragma unroll
    for (int i = 1; i < 16; ++i) mx = fmaxf(mx, S0[i]);
    if (mode1 != 2) {
#pragma unroll
      for (int i = 0; i < 16; ++i) mx = fmaxf(mx, S1[i]);
    }
    mx = fmaxf(mx, __shfl_xor(mx, 32));
    const float mn = fmaxf(st.m, mx);
    const float al = __builtin_amdgcn_exp2f(st.m - mn);
    st.m = mn;
    st.l *= al;
#pragma unroll
    for (int db = 0; db < 4; ++db)
#pragma unroll
      for (int i = 0; i < 16; ++i) st.O[db][i] *= al;
#pragma unroll
    for (int i = 0; i < 16; ++i) st.negm[i] = -mn;
  }
  pv_block(st, t + 16384, S0, st.m);
  if (mode1 != 2) pv_block(st, t + 16384 + 8192, S1, st.m);
}
__device__ __forceinline__ void st_reset(AttnState& st) {
#pragma unroll
  for (int db = 0; db < 4; ++db)
#pragma unroll
    for (int i = 0; i < 16; ++i) st.O[db][i] = 0.f;
  st.l = 0.f; st.m = -1e30f;
#pragma unroll
  for (int i = 0; i < 16; ++i) st.negm[i] = 0.f;
}
__device__ __forceinline__ bool st_bad(const AttnState& st) {
  float chk = 0.f;
#pragma unroll
  for (int db = 0; db < 4; ++db)
#pragma unroll
    for (int i = 0; i < 16; ++i) chk = __builtin_fmaf(st.O[db][i], 0.f, chk);
  return !(st.l <= 1.0e12f) || !(chk == 0.f);
}

__device__ __forceinline__ void attn_phase(KvOut& kv, const Params& p, ldsp lds, int wid, int lane, int h, int j, size_t fkv, unsigned long long epoch) {
  const int L = 15 - j, E = j;
  const bool helper = wid >= 4;
  const int w4 = wid & 3;
  const int Rown = helper ? 128 * E + 32 * w4 : 128 * L + 32 * w4;
  const int Rlate = 128 * L + 32 * w4;
  const rsrc_t rX = mk_rsrc(p.x, S_LEN * DA * 2);
  const rsrc_t rQ = mk_rsrc(p.wq, DA * 4096 * 2);
  const rsrc_t rKV = mk_rsrc(p.ws + WS_KV, 32u << 20);
  const rsrc_t rCtx = mk_rsrc(p.ws + WS_CTX, 16u << 20);
  unsigned sX = Rown * 1152, sWq = (8 * wid) * 8192 + h * 256, wl = wid * 2048;
  const int n = 2 * E + 2, NU = 34 - n;
  unsigned vL = lane * 16;
  unsigned vC = ((lane >> 4) & 1) * 1024 + (lane & 15) * 16 + (lane >> 5) * 256;
  auto issue_unit = [&](int u) {
    const int tile = u < n ? u : (((u - n) & 1) ? 17 + ((u - n) >> 1) : n + ((u - n) >> 1));
    const unsigned so = (h * 32 + tile) * 32768 + wid * 4096;
    dma16x4((u & 3) * 32768 + wid * 4096, vL, rKV, so);
  };
  auto store_ctx = [&](const f32x16& o, int row0, int db, float sc) {
#pragma unroll
    for (int s = 0; s < 2; ++s)
      st_wt(__builtin_bit_cast(u32x4, pack8(o, s, sc)), rCtx, (((row0 >> 5) * 32 + h) * 8 + db * 2) * 1024 + s * 512 + vC);
  };

  bool safe = false;
  for (int attempt = 0; attempt < 2; ++attempt) {
    AttnState st;
    sX = opaque_s(sX); sWq = opaque_s(sWq); wl = opaque_s(wl);
    lane = opaque_v(lane); vL = lane * 16;
    const int r32 = lane & 31, hh = lane >> 5;
    const unsigned vX0 = (lane >> 3) * 1152 + (((lane & 7) ^ (lane >> 4)) << 4);
    const unsigned vX1 = (lane >> 3) * 1152 + ((((lane & 7) ^ (lane >> 4)) ^ 4) << 4);
    const unsigned vWq = (lane >> 4) * 8192 + ((((lane & 15) >> 2) ^ (lane >> 4)) << 6) + ((lane & 3) << 4);
    const unsigned aX = wid * 4096 + r32 * 128 + ((hh ^ ((r32 >> 1) & 7)) << 4);
    const int q4 = (lane & 15) >> 2;
    const unsigned aWq = 32768 + (8 * hh + q4) * 256 + ((lane >> 4) & 1) * 32 + (lane & 3) * 8;
    auto dmaQ = [&](int kk, unsigned slot, int i) {
      if (i < 4) dma16(slot + 2 * wl + i * 1024, (i & 1) ? vX1 : vX0, rX, sX + i * 8 * 1152 + kk * 128);
      else dma16(slot + 32768 + wl + (i - 4) * 1024, vWq, rQ, sWq + (kk * 64 + 4 * (i - 4)) * 8192);
    };
#pragma unroll
    for (int k = 0; k < 3; ++k)
#pragma unroll
      for (int i = 0; i < 6; ++i) dmaQ(k, k * 49152, i);
    f32x16 qa[4];
#pragma unroll
    for (int db = 0; db < 4; ++db)
#pragma unroll
      for (int e = 0; e < 16; ++e) qa[db][e] = 0.f;
    wait_vm<12>(); bar();
    bf16x8 xf[2], wf[2][4];
    auto rdq = [&](int buf, unsigned slot, int ks, int part) {
      if (part == 0) xf[buf] = lds_b128(lds + (slot + (aX ^ (ks * 32))));
#pragma unroll
      for (int db = 0; db < 4; ++db)
        if ((db >> 1) == part) { ldsp bw = lds + (slot + aWq + ((db ^ q4) << 6)) + ks * 4096; wf[buf][db] = lds_tr2(bw, bw + 1024); }
    };
    unsigned slot = 0, pslot = 0;
    rdq(0, slot, 0, 0); rdq(0, slot, 0, 1);
    auto stkv = [&](auto I) {
      constexpr int i = I.value;
      __builtin_amdgcn_raw_buffer_store_b128(kv.v[i], rKV, kv.base, (i >> 3) * 32768 + ((i >> 2) & 1) * 8192 + (i & 3) * 1024, 16);
    };
    auto qstep = [&](auto K, auto VM) {
      constexpr int k = K.value;
#pragma unroll
      for (int ks = 0; ks < 3; ++ks) {
#pragma unroll
        for (int db = 0; db < 4; ++db) {
          if (db < 2) rdq((ks + 1) & 1, slot, ks + 1, db);
          const int sl = ks * 4 + db;
          if constexpr (k >= 1 && k <= 6) { if (sl % 3 == 0) dmaQ(k + 2, pslot, 2 + sl / 3); }
          if constexpr (k <= 1) {
            static_for<0, 8>([&](auto I) { if (sl == 1 + I.value + I.value / 2) stkv(IntC<8 * k + I.value>{}); });
          }
          qa[db] = mfma32(wf[ks & 1][db], xf[ks & 1], qa[db]);
          __builtin_amdgcn_sched_barrier(0);
        }
      }
      wait_lgkm0();
      if constexpr (k <= 7) wait_vm<VM.value>();
      if constexpr (k == 7) { if (wid == 0) poll_flags(p.ws, WS_FKV + (h >> 2) * 32 * 8, 32, epoch, lane); }
      bar();
      if constexpr (k == 3) raise_flag(p.ws, fkv, epoch);
      const unsigned os = slot;
      pslot = os;
      slot += 49152; if (slot >= 147456) slot = 0;
      __builtin_amdgcn_sched_barrier(0);
#pragma unroll
      for (int db = 0; db < 4; ++db) {
        if constexpr (k < 8) { if (db < 2) rdq(0, slot, 0, db); }
        if constexpr (k <= 5) { if (db == 0) dmaQ(k + 3, os, 0); if (db == 2) dmaQ(k + 3, os, 1); }
        if constexpr (k == 7) { if (db < 3) issue_unit(db); }
        if constexpr (k == 8) { if (db == 0) issue_unit(3); }
        qa[db] = mfma32(wf[1][db], xf[1], qa[db]);
        __builtin_amdgcn_sched_barrier(0);
      }
    };
    qstep(IntC<0>{}, IntC<14>{});
    qstep(IntC<1>{}, IntC<22>{});
    qstep(IntC<2>{}, IntC<8>{});
    qstep(IntC<3>{}, IntC<6>{});
    qstep(IntC<4>{}, IntC<6>{});
    qstep(IntC<5>{}, IntC<6>{});
    qstep(IntC<6>{}, IntC<6>{});
    qstep(IntC<7>{}, IntC<0>{});
    qstep(IntC<8>{}, IntC<0>{});
#pragma unroll
    for (int i = 0; i < 16; ++i) kv.v[i] = u32x4{0u, 0u, 0u, 0u};
    kv.base = 0x80000000u;
    const float qs = 0.08838834764831845f * 1.4426950408889634f;
#pragma unroll
    for (int ks = 0; ks < 8; ++ks) st.q[ks] = pack8(qa[ks >> 1], ks & 1, qs);
    lane = opaque_v(lane); vL = lane * 16;
    vC = ((lane >> 4) & 1) * 1024 + (lane & 15) * 16 + (lane >> 5) * 256;
    ldsp stash = lds + 131072 + w4 * 8192 + vL;
    if (!helper) {
#pragma unroll
      for (int ks = 0; ks < 8; ++ks) *(LAS bf16x8*)(stash + ks * 1024) = st.q[ks];
    }
    bool bad = false;
    int issued = 4;
    int nwin = 0;
    unsigned so0 = 0, so1 = 0, lo0 = 0, lo1 = 0;
    auto tile_of = [&](int u) { const int v = u - n; const int t2 = ((v & 1) ? 17 : n) + (v >> 1); return u < n ? u : t2; };
    auto sync = [&](int i) -> ldsp {
      const int uf = i < n ? i : n + 2 * (i - n);
      const int outst = issued - (uf + (i >= n ? 2 : 1));
      wait_vm_units(outst);
      bar();
      const int pend = issued;
      issued = uf + 4 < NU ? uf + 4 : NU;
      nwin = issued - pend;
      so0 = (h * 32 + tile_of(pend)) * 32768 + wid * 4096;
      so1 = (h * 32 + tile_of(pend + 1)) * 32768 + wid * 4096;
      lo0 = (pend & 3) * 32768 + wid * 4096;
      lo1 = ((pend + 1) & 3) * 32768 + wid * 4096;
      return lds + (((helper && i >= n) ? uf + 1 : uf) & 3) * 32768 + vL;
    };
    auto piece = [&](auto K) {
      constexpr int k = K.value;
      dma16_if<(k >> 2), (k & 3) * 1024>(nwin, k < 4 ? lo0 : lo1, vL, rKV, k < 4 ? so0 : so1);
    };
    auto flush = [&](auto K0) {
      if constexpr (K0.value == 0) { piece(IntC<0>{}); piece(IntC<1>{}); }
      piece(IntC<2>{}); piece(IntC<3>{}); piece(IntC<4>{}); piece(IntC<5>{}); piece(IntC<6>{}); piece(IntC<7>{});
      nwin = 0;
    };
    wait_lgkm0();
    ldsp t = sync(0);
    nwin = 0;
    int i = 0;
#pragma unroll 1
    for (int seg = helper ? 0 : 1; seg < 2; ++seg) {
      const int send = seg == 0 ? n : 17;
      const int toff = (helper && seg == 1) ? 17 - n : 0;
      const int rb = (seg == 0 ? Rown : Rlate) >> 5;
      if (helper && seg == 1) {
#pragma unroll
        for (int ks = 0; ks < 8; ++ks) st.q[ks] = *(const LAS bf16x8*)(stash + ks * 1024);
      }
      st_reset(st);
      auto gen_step = [&](bool upd) {
        const int kb0 = 2 * (i + toff);
        if (kb0 <= rb) unit_gen(st, t, lane, upd, kb0 == rb, kb0 + 1 < rb ? 0 : (kb0 + 1 == rb ? 1 : 2));
        ++i;
        wait_lgkm0();
        if (i < 17) { t = sync(i); flush(IntC<0>{}); }
      };
      gen_step(true);
      int fend = ((rb >> 1) - toff);
      fend = fend < send ? fend : send;
      if (safe) fend = 0;
      if (i < fend) {
        bf16x8 F0 = lds_b128(t + slot_off(0)), F1 = lds_b128(t + slot_off(1)), F2 = lds_b128(t + slot_off(2));
#pragma unroll 1
        do {
          ++i;
          unit_fast(st, t, F0, F1, F2, [&]() -> ldsp { if (i < 17) t = sync(i); else nwin = 0; return t; }, piece);
        } while (i < fend);
        flush(IntC<2>{});
      }
#pragma unroll 1
      while (i < send) gen_step(safe);
      bad |= st_bad(st);
      if (seg == 0) {
        const float inv = 1.0f / (st.l + xhalf(st.l));
#pragma unroll
        for (int db = 0; db < 4; ++db) store_ctx(st.O[db], Rown, db, inv);
      }
    }
    {
      const int xx = h >> 2, ss = j * 4 + (h & 3), tl = wid * 64 + lane;
      const rsrc_t rWo = mk_rsrc(p.wo, 4096 * DA * 2);
      const unsigned scratch = ((j & 1) ? 110592 : 157696) + wid * 256;
      const int c5 = tl % 5, r5 = tl / 5;
      touch4(scratch, r5 < 64 ? ((8 * (xx >> 1) + j) * 128 + r5) * 1152 + (h & 3) * 288 + c5 * 64 : 0x80000000u, rWo);
      touch4(scratch, r5 < 64 ? ((8 * (xx >> 1) + j) * 128 + 64 + r5) * 1152 + (h & 3) * 288 + c5 * 64 : 0x80000000u, rWo);
      touch4(scratch, tl < 128 ? (xx * 36 + ss) * 8192 + tl * 64 : (tl < 256 && ss >= 28 ? (xx * 36 + 4 + ss) * 8192 + (tl - 128) * 64 : 0x80000000u),
             mk_rsrc(p.assm, DA * S_LEN * 2));
      touch4(scratch, tl < 192 ? (ss * 64 + tl / 3) * 1152 + xx * 144 + (tl % 3) * 64 : 0x80000000u, mk_rsrc(p.aq, S_LEN * DA * 2));
      unsigned long long pc; unsigned len;
      asm volatile("s_getpc_b64 %0\nkf_here_%=:\n\ts_mov_b32 %1, kf_code_end-kf_here_%=" : "=s"(pc), "=s"(len));
      touch4(scratch, tl < 32 ? (ss * 32 + tl) * 64 : 0x80000000u, mk_rsrc((const void*)pc, len));
    }
    lane = opaque_v(lane); vL = lane * 16;
    vC = ((lane >> 4) & 1) * 1024 + (lane & 15) * 16 + (lane >> 5) * 256;
    float l = st.l;
    l += xhalf(l);
    wait_lgkm0(); bar();
    {
      ldsp ob = lds + wid * 8192 + vL;
      auto put = [&](const f32x16& o, int d2) {
#pragma unroll
        for (int c = 0; c < 4; ++c) *(LAS f32x4*)(ob + (d2 * 4 + c) * 1024) = f32x4{o[4 * c], o[4 * c + 1], o[4 * c + 2], o[4 * c + 3]};
      };
      if (helper) { put(st.O[0], 0); put(st.O[1], 1); } else { put(st.O[2], 0); put(st.O[3], 1); }
      *(LAS f32x2*)(lds + 65536 + wid * 512 + lane * 8) = f32x2{st.m, l};
      const bool anybad = __any(bad);
      if (lane == 0) *(LAS unsigned*)(lds + 69632 + wid * 4) = anybad ? 1u : 0u;
    }
    wait_lgkm0(); bar();
    const int pw = wid ^ 4;
    const f32x2 ml = *(const LAS f32x2*)(lds + 65536 + pw * 512 + lane * 8);
    const u32x4 b0 = *(const LAS u32x4*)(lds + 69632), b1 = *(const LAS u32x4*)(lds + 69648);
    const bool redo = __builtin_amdgcn_readfirstlane((b0[0] | b0[1] | b0[2] | b0[3] | b1[0] | b1[1] | b1[2] | b1[3])) != 0;
    if (!redo || safe) {
      const float mm = fmaxf(st.m, ml[0]);
      const float a = ex2(st.m - mm), bb = ex2(ml[0] - mm);
      const float inv = 1.0f / (a * l + bb * ml[1]);
      const float ca = a * inv, cb = bb * inv;
      ldsp ob = lds + pw * 8192 + vL;
      auto fin = [&](f32x16 o, int d2, int db) {
#pragma unroll
        for (int c = 0; c < 4; ++c) {
          const f32x4 v = *(const LAS f32x4*)(ob + (d2 * 4 + c) * 1024);
#pragma unroll
          for (int e = 0; e < 4; ++e) o[4 * c + e] = o[4 * c + e] * ca + v[e] * cb;
        }
        store_ctx(o, Rlate, db, 1.0f);
      };
      if (helper) { fin(st.O[2], 0, 2); fin(st.O[3], 1, 3); } else { fin(st.O[0], 0, 0); fin(st.O[1], 1, 1); }
      break;
    }
    safe = true;
    wait_lgkm0(); bar();
  }
}

struct A1Pre { u32x4 kr[5], vr[2], sh[4]; };
__device__ __forceinline__ void ldg16_async(u32x4& v, rsrc_t r, unsigned voff) {
  asm volatile("buffer_load_dwordx4 %0, %1, %2, 0 offen" : "=v"(v) : "v"(voff), "s"(r) : "memory");
}
__device__ __forceinline__ void a1_load(A1Pre& a, const Params& p, int tid, int dt, int tt) {
  const rsrc_t rK = mk_rsrc(p.ak, S_LEN * DA * 2), rV = mk_rsrc(p.av, S_LEN * DA * 2), rS = mk_rsrc(p.ashift, DA * 8);
  const int c = tid % 9, rp = tid / 9;
  const int t0 = tt * 64 + rp * 2, d0 = dt * 72 + c * 8;
  const bool on = tid < 288;
#pragma unroll
  for (int i = 0; i < 5; ++i) {
    const int t = t0 - 3 + i;
    ldg16_async(a.kr[i], rK, on && t >= 0 ? (unsigned)(t * DA + d0) * 2 : 0x80000000u);
  }
#pragma unroll
  for (int i = 0; i < 2; ++i) ldg16_async(a.vr[i], rV, on ? (unsigned)((t0 + i) * DA + d0) * 2 : 0x80000000u);
#pragma unroll
  for (int i = 0; i < 4; ++i) ldg16_async(a.sh[i], rS, on ? (unsigned)d0 * 8 + i * 16 : 0x80000000u);
}
template <int N>
__device__ __forceinline__ void a1_arrive(A1Pre& a, unsigned long long& e) {
  wait_vm<N>();
  asm volatile("" : "+v"(a.kr[0]), "+v"(a.kr[1]), "+v"(a.kr[2]), "+v"(a.kr[3]), "+v"(a.kr[4]), "+v"(a.vr[0]), "+v"(a.vr[1]),
                    "+v"(a.sh[0]), "+v"(a.sh[1]), "+v"(a.sh[2]), "+v"(a.sh[3]), "+v"(e));
}
__device__ __forceinline__ void a1_phase(const A1Pre& a, const Params& p, ldsp lds, int tid, int dt, int tt) {
  const rsrc_t rO = mk_rsrc(p.ws + WS_KVR, DA * KVR_PITCH * 2);
  if (tid < 288) {
    const int c = tid % 9, rp = tid / 9;
    const u32x4 *kr = a.kr, *vr = a.vr, *sh = a.sh;
#pragma unroll
    for (int e = 0; e < 8; ++e) {
      const unsigned s01 = sh[e >> 1][(e & 1) * 2], s23 = sh[e >> 1][(e & 1) * 2 + 1];
      const float w[4] = {bflo(s01), bfhi(s01), bflo(s23), bfhi(s23)};
      float r[2];
#pragma unroll
      for (int i = 0; i < 2; ++i) {
        float ks = 0.f;
#pragma unroll
        for (int t = 0; t < 4; ++t) {
          const unsigned kw = kr[i + t][e >> 1];
          ks = rbf(ks + rbf(((e & 1) ? bfhi(kw) : bflo(kw)) * w[3 - t]));
        }
        const unsigned vw = vr[i][e >> 1];
        r[i] = ks * ((e & 1) ? bfhi(vw) : bflo(vw));
      }
      *(LAS unsigned*)(lds + (c * 8 + e) * 132 + (31 - rp) * 4) = pk2(r[1], r[0]);
    }
  }
  wait_lgkm0(); bar();
#pragma unroll
  for (int i2 = 0; i2 < 2; ++i2) {
    const int idx = tid + i2 * 512, d = idx < 576 ? idx >> 3 : 0, ch = idx & 7;
    u32x4 v;
#pragma unroll
    for (int i = 0; i < 4; ++i) v[i] = *(const LAS unsigned*)(lds + d * 132 + ch * 16 + i * 4);
    if (idx >= 576) v = u32x4{0, 0, 0, 0};
    const unsigned off = idx < 576 ? (unsigned)((dt * 72 + d) * KVR_PITCH + (1984 - 64 * tt) + ch * 8) * 2
                                   : (unsigned)((dt * 72 + idx - 576) * KVR_PITCH + 2048 + 8 * tt) * 2;
    st_wt(v, rO, idx < 576 + 72 ? off : 0x80000000u);
  }
}


struct A2Pre { u32x4 h; unsigned hn; float D; };
constexpr unsigned A2_KVL = 147456;
__device__ __forceinline__ void a2_load_kv(const Params& p, int wid, int lane, int pair) {
  const rsrc_t rKv = mk_rsrc(p.ws + WS_KVR, DA * KVR_PITCH * 2);
  dma16_dev(A2_KVL + wid * 1024, lane * 16, rKv, pair * (2 * KVR_PITCH * 2) + wid * 1024);
  if (wid == 0) dma16_dev(A2_KVL + 8192, lane * 16, rKv, pair * (2 * KVR_PITCH * 2) + 8192);
}
__device__ __forceinline__ void a2_load_taps(A2Pre& pre, const Params& p, int wid, int lane, int pair) {
  const int d = pair * 2 + (wid >> 2);
  const rsrc_t rH = mk_rsrc(p.assm, DA * S_LEN * 2);
  const unsigned ho = (unsigned)(d * S_LEN + (wid & 3) * 512 + lane * 8) * 2;
  pre.h = __builtin_bit_cast(u32x4, ldg16(rH, ho, 0));
  pre.hn = __builtin_amdgcn_raw_buffer_load_b32(rH, ho + 16, 0, 0);
  pre.D = bf2f(__builtin_bit_cast(unsigned short, p.aD[d]));
}
template <class Mid>
__device__ __forceinline__ void a2_compute(const A2Pre& pre, const Params& p, ldsp lds, int wid, int lane, int pair, Mid&& mid) {
  const int ch = wid >> 2, wq = wid & 3, d = pair * 2 + ch;
  const rsrc_t rY = mk_rsrc(p.ws + WS_Y2, DA * S_LEN * 2);
  const unsigned red = ch ? 110592 : 36864;
  ldsp h0 = lds + red, h1 = lds + red + 4224;
  {
    const int e = wq * 64 + lane;
    *(LAS u32x4*)(h0 + 32 + e * 16) = pre.h;
    const u32x4 sh = {__builtin_amdgcn_alignbit(pre.h[1], pre.h[0], 16), __builtin_amdgcn_alignbit(pre.h[2], pre.h[1], 16),
                      __builtin_amdgcn_alignbit(pre.h[3], pre.h[2], 16), __builtin_amdgcn_alignbit(pre.hn, pre.h[3], 16)};
    *(LAS u32x4*)(h1 + 32 + e * 16) = sh;
    if (e < 8) *(LAS unsigned*)(h0 + e * 4) = 0u;
    if (e < 7) *(LAS unsigned*)(h1 + e * 4) = 0u;
    if (e == 0) *(LAS unsigned*)(h1 + 28) = pre.h[0] << 16;
  }
  wait_lgkm0(); bar();
  const int l15 = lane & 15, g = lane >> 4;
  ldsp kvl = lds + A2_KVL + ch * (KVR_PITCH * 2);
  f32x4 acc[8];
#pragma unroll
  for (int nb = 0; nb < 8; ++nb) acc[nb] = f32x4{0.f, 0.f, 0.f, 0.f};
#pragma unroll
  for (int rr = 0; rr < 2; ++rr) {
    const int u0 = 32 * (2 * wq + rr) - 15 + 8 * g + l15;
    ldsp ab = ((u0 & 1) ? h1 : h0) + 32 + (u0 >> 1) * 4;
    bf16x8 A[8], B[8];
#pragma unroll
    for (int mu = 0; mu < 8; ++mu) {
      const LAS unsigned* q = (const LAS unsigned*)(ab + mu * 512);
      const u32x4 v = {q[0], q[1], q[2], q[3]};
      A[mu] = __builtin_bit_cast(bf16x8, v);
    }
    ldsp bb = kvl + (2032 + 32 * (2 * wq + rr) - 16 * l15 + 8 * g) * 2;
#pragma unroll
    for (int D = 0; D < 8; ++D) B[D] = lds_b128(bb - 512 * D);
#pragma unroll
    for (int mu = 0; mu < 8; ++mu)
#pragma unroll
      for (int D = 0; D + mu < 8; ++D) acc[mu + D] = mfma16(A[mu], B[D], acc[mu + D]);
  }
  u32x2 kw2[2];
#pragma unroll
  for (int u = 0; u < 2; ++u) kw2[u] = *(const LAS u32x2*)(kvl + (2044 - (256 * (2 * wq + u) + 16 * l15 + 4 * g)) * 2);
  wait_lgkm0();
  mid();
#pragma unroll
  for (int nb = 0; nb < 8; ++nb) *(LAS f32x4*)(lds + red + wq * 8192 + nb * 1024 + lane * 16) = acc[nb];
  wait_lgkm0(); bar();
  u32x2 o[2];
#pragma unroll
  for (int u = 0; u < 2; ++u) {
    const int nb = 2 * wq + u;
    f32x4 y = *(const LAS f32x4*)(lds + red + nb * 1024 + lane * 16);
#pragma unroll
    for (int w = 1; w < 4; ++w) y += *(const LAS f32x4*)(lds + red + w * 8192 + nb * 1024 + lane * 16);
    const u32x2 kw = kw2[u];
    const float kv[4] = {bfhi(kw[1]), bflo(kw[1]), bfhi(kw[0]), bflo(kw[0])};
    float r[4];
#pragma unroll
    for (int e = 0; e < 4; ++e) r[e] = rbf(y[e]) + rbf(kv[e] * pre.D);
    o[u] = u32x2{pk2(r[0], r[1]), pk2(r[2], r[3])};
  }
  const u32x2 snd = (g & 1) ? o[0] : o[1];
  const u32x2 rcv = {(unsigned)__shfl_xor((int)snd[0], 16), (unsigned)__shfl_xor((int)snd[1], 16)};
  const u32x4 w = (g & 1) ? u32x4{rcv[0], rcv[1], o[1][0], o[1][1]} : u32x4{o[0][0], o[0][1], rcv[0], rcv[1]};
  st_wt(w, rY, (unsigned)(d * S_LEN + 256 * (2 * wq + (g & 1)) + 16 * l15 + 8 * (g >> 1)) * 2);
  wait_lgkm0(); bar();
}

struct A3Pre { u32x4 q[2], y[2]; };
__device__ __forceinline__ void a3_loadq(A3Pre& r, const Params& p, int tid, int dt, int tt) {
  const rsrc_t rQ = mk_rsrc(p.aq, S_LEN * DA * 2);
  const int c = tid % 9, rp = tid / 9;
#pragma unroll
  for (int i = 0; i < 2; ++i)
    r.q[i] = __builtin_bit_cast(u32x4, ldg16(rQ, tid < 288 ? (unsigned)((tt * 64 + rp * 2 + i) * DA + dt * 72 + c * 8) * 2 : 0x80000000u, 0));
}
__device__ __forceinline__ void a3_loady(A3Pre& r, const Params& p, int tid, int dt, int tt) {
  const rsrc_t rY = mk_rsrc(p.ws + WS_Y2, DA * S_LEN * 2);
#pragma unroll
  for (int i = 0; i < 2; ++i) {
    const int idx = tid + i * 512;
    r.y[i] = __builtin_bit_cast(u32x4, ldg16_dev(rY, idx < 576 ? (unsigned)((dt * 72 + (idx >> 3)) * S_LEN + tt * 64 + (idx & 7) * 8) * 2 : 0x80000000u, 0));
  }
}
__device__ __forceinline__ void a3_finish(const A3Pre& r, const Params& p, ldsp lds, int tid, int dt, int tt) {
  const rsrc_t rO = mk_rsrc(p.out, S_LEN * OUTW * 2);
  const int c = tid % 9, rp = tid / 9;
#pragma unroll
  for (int i = 0; i < 2; ++i) {
    const int idx = tid + i * 512;
    if (idx < 576) {
#pragma unroll
      for (int e = 0; e < 4; ++e) *(LAS unsigned*)(lds + (idx >> 3) * 132 + (idx & 7) * 16 + e * 4) = r.y[i][e];
    }
  }
  wait_lgkm0(); bar();
  if (tid < 288) {
    float y[8][2];
#pragma unroll
    for (int e = 0; e < 8; ++e) {
      const unsigned w = *(const LAS unsigned*)(lds + (c * 8 + e) * 132 + rp * 4);
      y[e][0] = bflo(w); y[e][1] = bfhi(w);
    }
#pragma unroll
    for (int i = 0; i < 2; ++i) {
      u32x4 o;
#pragma unroll
      for (int e = 0; e < 4; ++e) o[e] = pk2(bflo(r.q[i][e]) * y[2 * e][i], bfhi(r.q[i][e]) * y[2 * e + 1][i]);
      st_wt(o, rO, (unsigned)((tt * 64 + rp * 2 + i) * OUTW + dt * 72 + c * 8) * 2);
    }
  }
}

constexpr unsigned OUT_OWN = 81920;
struct OutCtx { rsrc_t rWo; unsigned voff[9]; unsigned lbase, sbase; };
__device__ __forceinline__ void out_init(OutCtx& o, const Params& p, int wid, int lane, int ks, int nt) {
  o.rWo = mk_rsrc(p.wo, 4096 * DA * 2);
#pragma unroll
  for (int i = 0; i < 9; ++i) { const int idx = i * 64 + lane; o.voff[i] = (idx / 18) * 1152 + (idx % 18) * 16; }
  o.lbase = (wid >> 2) * 73728 + (wid & 3) * 9216;
  o.sbase = ((8 * ks + (wid >> 2)) * 128 + 32 * (wid & 3)) * 1152 + nt * 288;
}
__device__ __forceinline__ void out_dma(const OutCtx& o, int step, int i) {
  dma16(o.lbase + (step & 1) * 36864 + i * 1024, o.voff[i], o.rWo, o.sbase + step * (256 * 1152));
}
struct OutB { bf16x8 f[3][8]; };
__device__ __forceinline__ void out_loadB(OutB& B, const Params& p, int wid, int lane, int ks, int blk, int head2) {
  const rsrc_t rCtx = mk_rsrc(p.ws + WS_CTX, 16u << 20);
  const unsigned sB = ((blk * 4 + (wid & 3)) * 32 + 8 * ks + (wid >> 2)) * 8192;
#pragma unroll
  for (int f = 0; f < 8; ++f) B.f[head2][f] = ldg16_dev(rCtx, lane * 16 + f * 1024, sB + head2 * 16384);
}
template <class Hook>
__device__ __forceinline__ void out_gemm(const Params& p, const OutCtx& o, OutB& B, ldsp lds, int wid, int lane, int ks, int blk, int grp, Hook&& hook) {
  const int mt = wid & 3, kg = wid >> 2;
  const rsrc_t rCtx = mk_rsrc(p.ws + WS_CTX, 16u << 20);
  const unsigned sB = ((blk * 4 + mt) * 32 + 8 * ks + kg) * 8192;
  const unsigned vL = lane * 16;
  const int highhalf = __builtin_amdgcn_readfirstlane(wid & 2), lowhalf = __builtin_amdgcn_readfirstlane(~wid & 2);
  f32x4 acc[9][2];
#pragma unroll
  for (int nb = 0; nb < 9; ++nb) { acc[nb][0] = f32x4{0.f, 0.f, 0.f, 0.f}; acc[nb][1] = acc[nb][0]; }
  const int g = lane >> 4;
  const unsigned aA = kg * 73728 + (16 * (g >> 1) + 4 * (g & 1) + ((lane & 15) >> 2)) * 288 + (lane & 3) * 8;
  auto dmaW = [&](int step, int i, int on) {
    dma16_on(on, o.lbase + (step & 1) * 36864 + i * 1024, o.voff[i], o.rWo, o.sbase + step * (256 * 1152));
  };
  static_for<0, 4>([&](auto SI) {
    constexpr int st = SI.value;
    if constexpr (st <= 1) wait_vm<8>();
    else if constexpr (st == 2) wait_vm<12>();
    else { if (highhalf) wait_vm<9>(); else wait_vm<0>(); }
    bar();
    if constexpr (st == 0) hook();
    ldsp aw = lds + aA + (st & 1) * 36864;
    bf16x8 A[40];
    auto rdA = [&](auto K) { constexpr int k = K.value; ldsp q = aw + (k / 9) * 9216 + (k % 9) * 32; A[k] = lds_tr2(q, q + 2304); };
    rdA(IntC<0>{}); rdA(IntC<1>{}); rdA(IntC<2>{}); rdA(IntC<3>{});
    static_for<0, 18>([&](auto KI) {
      constexpr int k2 = KI.value, k = 2 * k2;
      if constexpr (k == 14) {
        wait_lgkm0();
        if constexpr (st == 2) { if (highhalf) wait_vm<2>(); }
        if constexpr (st == 3) { if (highhalf) wait_vm<0>(); }
        bar();
      }
      if constexpr (k + 4 < 36) { rdA(IntC<k + 4>{}); rdA(IntC<k + 5>{}); }
      if constexpr (st == 0 && k2 < 8) B.f[2][k2] = ldg16_dev(rCtx, vL + k2 * 1024, sB + 2 * 16384);
      if constexpr (st == 0 && k2 >= 9) dmaW(2, k2 - 9, lowhalf);
      if constexpr (st == 1 && (k2 & 1) == 0) dmaW(2, k2 / 2, highhalf);
      if constexpr (st == 1 && (k2 & 1) == 1 && k2 < 16) B.f[0][k2 / 2] = ldg16_dev(rCtx, vL + (k2 / 2) * 1024, sB + 3 * 16384);
      if constexpr (st == 1 && k2 >= 10 && (k2 & 1) == 0) dmaW(3, (k2 - 10) / 2, lowhalf);
      if constexpr (st == 2 && k2 < 5) dmaW(3, 4 + k2, lowhalf);
      if constexpr (st == 2 && (k2 == 5 || k2 == 6 || k2 >= 11)) dmaW(3, k2 < 7 ? k2 - 5 : k2 - 9, highhalf);
      acc[k % 9][0] = mfma16(A[k], B.f[st % 3][(k / 9) * 2], acc[k % 9][0]);
      acc[k % 9][1] = mfma16(A[k], B.f[st % 3][(k / 9) * 2 + 1], acc[k % 9][1]);
      acc[(k + 1) % 9][0] = mfma16(A[k + 1], B.f[st % 3][((k + 1) / 9) * 2], acc[(k + 1) % 9][0]);
      acc[(k + 1) % 9][1] = mfma16(A[k + 1], B.f[st % 3][((k + 1) / 9) * 2 + 1], acc[(k + 1) % 9][1]);
      SB();
    });
    wait_lgkm0();
  });
  bar();
#pragma unroll
  for (int nb = 0; nb < 9; ++nb) *(LAS f32x4*)(lds + wid * 9216 + nb * 1024 + vL) = kg ? acc[nb][0] : acc[nb][1];
  wait_lgkm0(); bar();
  const rsrc_t rP = mk_rsrc(p.ws + WS_PART, 64 * 4 * 73728);
#pragma unroll
  for (int nb = 0; nb < 9; ++nb) {
    const f32x4 v = (kg ? acc[nb][1] : acc[nb][0]) + *(const LAS f32x4*)(lds + (wid ^ 4) * 9216 + nb * 1024 + vL);
    const int owner = nb < 8 ? nb >> 1 : 3;
    if (owner == ks) *(LAS f32x4*)(lds + OUT_OWN + wid * 3072 + (nb - 2 * owner) * 1024 + vL) = v;
    else st_wt(__builtin_bit_cast(u32x4, v), rP, (grp * 4 + ks) * 73728 + wid * 9216 + nb * 1024 + vL);
  }
  wait_lgkm0(); bar();
}
__device__ __forceinline__ void out_final(const Params& p, ldsp lds, int wid, int lane, int ks, int blk, int nt, int grp) {
  const rsrc_t rP = mk_rsrc(p.ws + WS_PART, 64 * 4 * 73728);
  const rsrc_t rO = mk_rsrc(p.out, S_LEN * OUTW * 2);
  f32x4 v[3][4];
#pragma unroll
  for (int u = 0; u < 3; ++u)
#pragma unroll
    for (int src = 0; src < 4; ++src) {
      const bool fetch = src != ks && (u < 2 || ks == 3);
      v[u][src] = __builtin_bit_cast(f32x4, ldg16_dev(rP, fetch ? lane * 16 : 0x80000000u, (grp * 4 + src) * 73728 + wid * 9216 + (2 * ks + u) * 1024));
    }
#pragma unroll
  for (int u = 0; u < 3; ++u) {
    const f32x4 own = *(const LAS f32x4*)(lds + OUT_OWN + wid * 3072 + u * 1024 + lane * 16);
#pragma unroll
    for (int src = 0; src < 4; ++src) v[u][src] = src == ks ? own : v[u][src];
  }
  const int t = blk * 128 + (wid & 3) * 32 + (wid >> 2) * 16 + (lane & 15), g = lane >> 4;
  u32x2 o[3];
#pragma unroll
  for (int u = 0; u < 3; ++u) {
    const f32x4 r = (v[u][0] + v[u][1]) + (v[u][2] + v[u][3]);
    o[u] = u32x2{pk2(r[0], r[1]), pk2(r[2], r[3])};
  }
  const u32x2 snd = (g & 1) ? o[0] : o[1];
  const u32x2 rcv = {(unsigned)__shfl_xor((int)snd[0], 16), (unsigned)__shfl_xor((int)snd[1], 16)};
  const u32x4 w = (g & 1) ? u32x4{rcv[0], rcv[1], o[1][0], o[1][1]} : u32x4{o[0][0], o[0][1], rcv[0], rcv[1]};
  const unsigned rowb = (unsigned)(t * OUTW + DA + nt * 144 + 2 * ks * 16) * 2;
  st_wt(w, rO, rowb + (g & 1) * 32 + (g >> 1) * 16);
  if (ks == 3) __builtin_amdgcn_raw_buffer_store_b64(o[2], rO, rowb + 64 + g * 8, 0, 16);
}

__global__ __launch_bounds__(512, 2) void k_fused(Params p) {
  extern __shared__ __attribute__((aligned(16))) char smem[];
  ldsp lds = (ldsp)smem;
  const int wid = __builtin_amdgcn_readfirstlane(threadIdx.x >> 6);
  int lane = threadIdx.x & 63;
  const int b = blockIdx.x, x = b & 7, s = b >> 3;
  unsigned cw = 0;
  {
    unsigned long long pc; unsigned len;
    asm volatile("s_getpc_b64 %0\nkf_code_begin:\n\ts_mov_b32 %1, kf_code_end-kf_code_begin" : "=s"(pc), "=s"(len));
    const unsigned off = (s * 512 + threadIdx.x) * 64;
    if (off < len) asm volatile("global_load_dword %0, %1, %2" : "+v"(cw) : "v"(off), "s"(pc) : "memory");
  }
  unsigned long long epoch;
  A1Pre a1;
  KvOut kv;
  kv_dispatch(kv, p, lds, wid, lane, b, [&]() {
    asm volatile("global_load_dwordx2 %0, %1, %2 sc1" : "=v"(epoch) : "v"(0u), "s"(p.ws + WS_EPOCH) : "memory");
    a1_load(a1, p, threadIdx.x, x, s);
  }, [&]() {
    a1_arrive<16>(a1, epoch);
    epoch += 1;
    a1_phase(a1, p, lds + 131072, threadIdx.x, x, s);
  });
  asm volatile("" :: "v"(cw));
  const int h = 4 * x + (s & 3), j = s >> 2;
  attn_phase(kv, p, lds, wid, opaque_v(lane), h, j, WS_FKV + ((2 * x + (s & 1)) * 16 + (s >> 1)) * 8, epoch);
  lane = opaque_v(lane);
  const int ks = x >> 1, blk = (x & 1) ? 15 - j : j, nt = s & 3, grp = (j * 2 + (x & 1)) * 4 + nt;
  const int extra = s - 28;
  OutCtx o;
  out_init(o, p, wid, lane, ks, nt);
  A2Pre pre;
  wait_lgkm0(); bar();
#pragma unroll
  for (int i = 0; i < 9; ++i) out_dma(o, 0, i);
  a2_load_kv(p, wid, lane, x * 36 + s);
  a2_load_taps(pre, p, wid, lane, x * 36 + s);
  A2Pre pre2;
  if (extra >= 0) a2_load_taps(pre2, p, wid, lane, x * 36 + 32 + extra);
  publish(p.ws, WS_FATT + (j * 32 + h) * 8, epoch);
  OutB B;
  auto ctx_arrive = [&]() {
    await_flags(p.ws, WS_FATT + (j * 32 + 8 * ks) * 8, 8, epoch, wid, lane);
    out_loadB(B, p, wid, lane, ks, blk, 0);
    out_loadB(B, p, wid, lane, ks, blk, 1);
  };
  if (extra >= 0) {
    a2_compute(pre, p, lds, wid, lane, x * 36 + s, [&]() { bar(); a2_load_kv(p, wid, lane, x * 36 + 32 + extra); });
    wait_vm<0>(); bar();
    a2_compute(pre2, p, lds, wid, lane, x * 36 + 32 + extra, ctx_arrive);
  } else {
    a2_compute(pre, p, lds, wid, lane, x * 36 + s, ctx_arrive);
  }
#pragma unroll
  for (int i = 0; i < 9; ++i) out_dma(o, 1, i);
  out_gemm(p, o, B, lds, wid, lane, ks, blk, grp, [&]() {
    raise_flag(p.ws, WS_FA2 + (x * 64 + s) * 8, epoch);
    if (extra >= 0) raise_flag(p.ws, WS_FA2 + (x * 64 + 32 + extra) * 8, epoch);
  });
  A3Pre a3;
  a3_loadq(a3, p, threadIdx.x, x, s);
  await_flags(p.ws, WS_FA2 + x * 64 * 8, 36, epoch, wid, lane);
  a3_loady(a3, p, threadIdx.x, x, s);
  publish(p.ws, WS_FPART + (grp * 4 + ks) * 8, epoch);
  a3_finish(a3, p, lds, threadIdx.x, x, s);
  await_flags(p.ws, WS_FPART + grp * 4 * 8, 4, epoch, wid, lane);
  out_final(p, lds, wid, lane, ks, blk, nt, grp);
  if (b == 0 && threadIdx.x == 0) __hip_atomic_store((gu64*)(p.ws + WS_EPOCH), epoch, __ATOMIC_RELAXED, __HIP_MEMORY_SCOPE_AGENT);
  asm volatile("kf_code_end:");
}

static Params make_params(void* const* d_in, void* d_out, void* d_ws) {
  Params p;
  p.aq = (const __bf16*)d_in[0]; p.ak = (const __bf16*)d_in[1]; p.av = (const __bf16*)d_in[2];
  p.ashift = (const __bf16*)d_in[3]; p.assm = (const __bf16*)d_in[4]; p.aD = (const __bf16*)d_in[5];
  p.x = (const __bf16*)d_in[6]; p.ckv = (const __bf16*)d_in[7]; p.wq = (const __bf16*)d_in[8];
  p.wuk = (const __bf16*)d_in[9]; p.wuv = (const __bf16*)d_in[10]; p.wo = (const __bf16*)d_in[11];
  p.out = (__bf16*)d_out; p.ws = (char*)d_ws;
  return p;
}

extern "C" void kernel_launch(void* const* d_in, const int* in_sizes, int n_in,
                              void* d_out, int out_size, void* d_ws, size_t ws_size,
                              hipStream_t stream) {
  Params p = make_params(d_in, d_out, d_ws);
  (void)hipFuncSetAttribute((const void*)k_fused, hipFuncAttributeMaxDynamicSharedMemorySize, LDS_BYTES);
  k_fused<<<256, 512, LDS_BYTES, stream>>>(p);
}
```
